```python
import torch

def _softmax(x, axis=-1):
    return torch.softmax(x, dim=axis)
def _gelu(x):
    return torch.nn.functional.gelu(x, approximate="tanh")
def _silu(x):
    return torch.nn.functional.silu(x)
def _sigmoid(x):
    return torch.sigmoid(x)
def _layernorm(x, axis=-1, eps=1e-5):
    m = x.mean(dim=axis, keepdim=True)
    v = x.var(dim=axis, keepdim=True, unbiased=False)
    return (x - m) / torch.sqrt(v + eps)
def _scatter_add(tgt, idx, src, axis=0):
    return tgt.clone().index_add_(axis, idx, src)
def _segment_sum(src, seg, n):
    out = torch.zeros((n,) + src.shape[1:], dtype=src.dtype)
    return out.index_add_(0, seg, src)
def _pad(x, pw):
    flat = tuple(v for pair in reversed(pw) for v in pair)
    return torch.nn.functional.pad(x, flat)

kernel_name = 'sequential_preLN__sliding_block_cached__mixer_channel_mlp__decode'

def setup_inputs():

    _w = (lambda a: (lambda t: t.to(torch.bfloat16) if t.is_floating_point() else t)(torch.as_tensor(a)))
    def _A_setup():
        B, Sq, D = 8, 1, 512
        Hq, Hkv, Sk, Dh = 32, 8, 4096, 128
        assert Hq % Hkv == 0, (Hq, Hkv)
        return {
            "x": _w((lambda *s: torch.randn(*s, dtype=torch.bfloat16) * 0.05)(B, Sq, D)),
            "k_cache": _w((lambda *s: torch.randn(*s, dtype=torch.bfloat16) * 3.2)(B, Hkv, Sk, Dh)),
            "v_cache": _w((lambda *s: torch.randn(*s, dtype=torch.bfloat16) * 0.4)(B, Hkv, Sk, Dh)),
            "wq": _w((lambda *s: torch.randn(*s, dtype=torch.bfloat16) * 3.2)(D, Hq * Dh) * (D ** -0.5)),
            "wo": _w((lambda *s: torch.randn(*s, dtype=torch.bfloat16) * 0.4)(Hq * Dh, D) * ((Hq * Dh) ** -0.5)),
        }
    _A_in = _A_setup()

    def _B_setup():
        B, S, D, F = 8, 1, 512, 1792
        return {
            "x": _w((lambda *s: torch.randn(*s, dtype=torch.bfloat16) * 0.05)(B, S, D)),
            "w1": _w((lambda *s: torch.randn(*s, dtype=torch.bfloat16) * 3.2)(D, F) * (D ** -0.5)),
            "b1": _w((lambda *s: torch.randn(*s, dtype=torch.bfloat16) * 1.6)(F)),
            "w2": _w((lambda *s: torch.randn(*s, dtype=torch.bfloat16) * 0.2)(F, D) * (F ** -0.5)),
            "b2": _w((lambda *s: torch.randn(*s, dtype=torch.bfloat16) * 0.4)(D)),
        }
    _B_in = _B_setup()

    return {
        "A_x": _A_in["x"],
        "A_k_cache": _A_in["k_cache"],
        "A_v_cache": _A_in["v_cache"],
        "A_wq": _A_in["wq"],
        "A_wo": _A_in["wo"],
        "B_x": _B_in["x"],
        "B_w1": _B_in["w1"],
        "B_b1": _B_in["b1"],
        "B_w2": _B_in["w2"],
        "B_b2": _B_in["b2"],
    }

def reference(A_x, A_k_cache, A_v_cache, A_wq, A_wo, B_x, B_w1, B_b1, B_w2, B_b2):
    def _A_ref(x, k_cache, v_cache, wq, wo):
        B, Sq, D = x.shape
        Hq, Dh = 32, 128
        Hkv, Sk = k_cache.shape[1], k_cache.shape[2]
        W = min(4096, max(256, Sk // 4))
        rep = Hq // Hkv
        q = (lambda x, *d: x.permute(*d))((x @ wq).reshape(B, Sq, Hq, Dh), 0, 2, 1, 3)
        k = (lambda x, n, axis: torch.repeat_interleave(x, n, dim=axis))(k_cache[:, :, Sk - W:, :], rep, axis=1)
        v = (lambda x, n, axis: torch.repeat_interleave(x, n, dim=axis))(v_cache[:, :, Sk - W:, :], rep, axis=1)
        scores = torch.einsum("bhqd,bhkd->bhqk", q, k) * (Dh ** -0.5)
        p = _softmax(scores, axis=-1)
        o = torch.einsum("bhqk,bhkd->bhqd", p, v)
        return (lambda x, *d: x.permute(*d))(o, 0, 2, 1, 3).reshape(B, Sq, Hq * Dh) @ wo

    def _B_ref(x, w1, b1, w2, b2):
        y = _layernorm(x, axis=-1)
        h = _gelu(torch.einsum("bsd,df->bsf", y, w1) + b1)
        return torch.einsum("bsf,fd->bsd", h, w2) + b2

    x = A_x
    h = x + _A_ref(_layernorm(x, axis=-1), A_k_cache, A_v_cache, A_wq, A_wo)
    h2 = h + _B_ref(_layernorm(h, axis=-1), B_w1, B_b1, B_w2, B_b2)
    return h2
```

```cpp
#include <hip/hip_runtime.h>
#include <hip/hip_bf16.h>
#include <stdint.h>

typedef unsigned v4u __attribute__((ext_vector_type(4)));
typedef float f32x4 __attribute__((ext_vector_type(4)));
typedef short bf16x8 __attribute__((ext_vector_type(8)));
typedef unsigned short u16;

constexpr int B = 8, D = 512, HQ = 32, HKV = 8, SK = 4096, DH = 128, W = 1024, F = 1792;

__device__ __forceinline__ float bf2f(u16 b) { return __builtin_bit_cast(float, (unsigned)b << 16); }
__device__ __forceinline__ unsigned cvtpk(float lo, float hi) {
  unsigned r; asm("v_cvt_pk_bf16_f32 %0, %1, %2" : "=v"(r) : "v"(lo), "v"(hi)); return r;
}
__device__ __forceinline__ float rbf(float f) { return __builtin_bit_cast(float, cvtpk(f, f) << 16); }
__device__ __forceinline__ f32x4 mfma16(v4u a, v4u b, f32x4 c) {
  return __builtin_amdgcn_mfma_f32_16x16x32_bf16(__builtin_bit_cast(bf16x8, a), __builtin_bit_cast(bf16x8, b), c, 0, 0, 0);
}
__device__ __forceinline__ float wave_sum(float v) {
  for (int o = 32; o; o >>= 1) v += __shfl_xor(v, o);
  return v;
}
__device__ __forceinline__ void ln_row(const float (&x)[8], float (&y)[8]) {
  float s = 0.f;
  for (int i = 0; i < 8; ++i) s += x[i];
  s = wave_sum(s);
  const float mean = s * (1.f / 512.f);
  float v = 0.f;
  for (int i = 0; i < 8; ++i) { float d = x[i] - mean; v += d * d; }
  v = wave_sum(v) * (1.f / 512.f);
  const float mb = rbf(mean), vb = rbf(v);
  const float den = rbf(sqrtf(rbf(vb + 1e-5f)));
  for (int i = 0; i < 8; ++i) y[i] = rbf(rbf(x[i] - mb) / den);
}

__global__ void __launch_bounds__(256) k_q(const u16* x, const u16* wq, float* q) {
  __shared__ float xn[8][512];
  __shared__ float red[16][8][16];
  const int tid = threadIdx.x, lane = tid & 63, w = tid >> 6;
  for (int b = w; b < 8; b += 4) {
    float v[8], y[8];
    for (int i = 0; i < 8; ++i) v[i] = bf2f(x[b * 512 + lane * 8 + i]);
    ln_row(v, y);
    for (int i = 0; i < 8; ++i) xn[b][lane * 8 + i] = y[i];
  }
  __syncthreads();
  const int col = tid & 15, kp = tid >> 4;
  float acc[8] = {0};
  for (int k = kp * 32; k < kp * 32 + 32; ++k) {
    float wv = bf2f(wq[(size_t)k * 4096 + blockIdx.x * 16 + col]);
    for (int b = 0; b < 8; ++b) acc[b] += xn[b][k] * wv;
  }
  for (int b = 0; b < 8; ++b) red[kp][b][col] = acc[b];
  __syncthreads();
  if (tid < 128) {
    int b = tid >> 4, c = tid & 15; float s = 0;
    for (int i = 0; i < 16; ++i) s += red[i][b][c];
    q[b * 4096 + blockIdx.x * 16 + c] = rbf(s);
  }
}

__global__ void __launch_bounds__(512) k_attn(const u16* kc, const u16* vc, const float* q, float* po, float* pml) {
  __shared__ float so[8][512];
  __shared__ float sml[8][8];
  const int blk = blockIdx.x, g = blk & 7, r = blk >> 3, b = r >> 2, s = r & 3;
  const int tid = threadIdx.x, lane = tid & 63, w = tid >> 6;
  const int lg = lane >> 4, lc = lane & 15;
  const size_t base = ((size_t)(b * HKV + g) * SK + (SK - W) + s * 256 + w * 32) * DH;
  v4u Kr[8], Vr[8];
  for (int i = 0; i < 8; ++i) Kr[i] = __builtin_nontemporal_load((const v4u*)(kc + base + (size_t)(4 * i + (lc >> 2)) * DH + (4 * lg + (lc & 3)) * 8));
  for (int i = 0; i < 8; ++i) Vr[i] = __builtin_nontemporal_load((const v4u*)(vc + base + (size_t)(4 * i + lg) * DH + lc * 8));
  v4u Qf;
  {
    const float* qp = q + b * 4096 + g * 512 + (lc >> 2) * 128 + (4 * lg + (lc & 3)) * 8;
    Qf.x = cvtpk(qp[0], qp[1]); Qf.y = cvtpk(qp[2], qp[3]); Qf.z = cvtpk(qp[4], qp[5]); Qf.w = cvtpk(qp[6], qp[7]);
  }
  float sc[8];
  const int e = lc & 3;
  for (int i = 0; i < 8; ++i) {
    f32x4 d = mfma16(Kr[i], Qf, f32x4{0, 0, 0, 0});
    float v = e == 0 ? d[0] : e == 1 ? d[1] : e == 2 ? d[2] : d[3];
    v += __shfl_xor(v, 1); v += __shfl_xor(v, 2);
    sc[i] = rbf(rbf(v) * 0.08838834764831845f);
  }
  float m = sc[0];
  for (int i = 1; i < 8; ++i) m = fmaxf(m, sc[i]);
  m = fmaxf(m, __shfl_xor(m, 16)); m = fmaxf(m, __shfl_xor(m, 32));
  float p[8], l = 0;
  for (int i = 0; i < 8; ++i) { p[i] = __expf(sc[i] - m); l += p[i]; }
  l += __shfl_xor(l, 16); l += __shfl_xor(l, 32);
  v4u Pf; Pf.x = cvtpk(p[0], p[1]); Pf.y = cvtpk(p[2], p[3]); Pf.z = cvtpk(p[4], p[5]); Pf.w = cvtpk(p[6], p[7]);
  float o[8];
  for (int jj = 0; jj < 8; ++jj) {
    v4u Bf;
    const unsigned sel = (jj & 1) ? 0x07060302u : 0x05040100u;
    const int dw = jj >> 1;
    Bf.x = __builtin_amdgcn_perm(Vr[1][dw], Vr[0][dw], sel);
    Bf.y = __builtin_amdgcn_perm(Vr[3][dw], Vr[2][dw], sel);
    Bf.z = __builtin_amdgcn_perm(Vr[5][dw], Vr[4][dw], sel);
    Bf.w = __builtin_amdgcn_perm(Vr[7][dw], Vr[6][dw], sel);
    f32x4 d = mfma16(Pf, Bf, f32x4{0, 0, 0, 0});
    o[jj] = d[0];
  }
  for (int jj = 0; jj < 8; ++jj) so[w][lg * 128 + lc * 8 + jj] = o[jj];
  if (lg == 0 && e == 0) { sml[w][(lc >> 2) * 2] = m; sml[w][(lc >> 2) * 2 + 1] = l; }
  __syncthreads();
  {
    const int h = tid >> 7;
    float M = sml[0][2 * h];
    for (int ww = 1; ww < 8; ++ww) M = fmaxf(M, sml[ww][2 * h]);
    float acc = 0, L = 0;
    for (int ww = 0; ww < 8; ++ww) { float f = __expf(sml[ww][2 * h] - M); acc += f * so[ww][tid]; L += f * sml[ww][2 * h + 1]; }
    po[(size_t)blk * 512 + tid] = acc;
    if ((tid & 127) == 0) { pml[blk * 8 + 2 * h] = M; pml[blk * 8 + 2 * h + 1] = L; }
  }
}

__global__ void __launch_bounds__(256) k_wo(const float* po, const float* pml, const u16* wo, float* hp) {
  __shared__ float o[8][128];
  __shared__ float red[4][8][64];
  const int hh = blockIdx.x >> 3, cb = blockIdx.x & 7, g = hh >> 2, hl = hh & 3;
  const int tid = threadIdx.x;
  for (int idx = tid; idx < 1024; idx += 256) {
    int b = idx >> 7, d = idx & 127;
    float M = -INFINITY;
    for (int s = 0; s < 4; ++s) M = fmaxf(M, pml[(((b * 4 + s) << 3) + g) * 8 + 2 * hl]);
    float acc = 0, L = 0;
    for (int s = 0; s < 4; ++s) { int blk = ((b * 4 + s) << 3) + g; float f = __expf(pml[blk * 8 + 2 * hl] - M); acc += f * po[(size_t)blk * 512 + hl * 128 + d]; L += f * pml[blk * 8 + 2 * hl + 1]; }
    o[b][d] = rbf(acc / L);
  }
  __syncthreads();
  const int col = tid & 63, kp = tid >> 6;
  float acc[8] = {0};
  for (int k = kp * 32; k < kp * 32 + 32; ++k) {
    float wv = bf2f(wo[(size_t)(hh * 128 + k) * 512 + cb * 64 + col]);
    for (int b = 0; b < 8; ++b) acc[b] += o[b][k] * wv;
  }
  for (int b = 0; b < 8; ++b) red[kp][b][col] = acc[b];
  __syncthreads();
  for (int idx = tid; idx < 512; idx += 256) {
    int b = idx >> 6, c = idx & 63;
    hp[((size_t)hh * 8 + b) * 512 + cb * 64 + c] = red[0][b][c] + red[1][b][c] + red[2][b][c] + red[3][b][c];
  }
}
__global__ void __launch_bounds__(64) k_h(const u16* x, const float* hp, float* h, float* y) {
  const int b = blockIdx.x, lane = threadIdx.x;
  float v[8], yy[8];
  for (int i = 0; i < 8; ++i) {
    float s = 0;
    for (int hh = 0; hh < 32; ++hh) s += hp[((size_t)hh * 8 + b) * 512 + lane * 8 + i];
    v[i] = rbf(bf2f(x[b * 512 + lane * 8 + i]) + rbf(s));
  }
  ln_row(v, yy);
  for (int i = 0; i < 8; ++i) { h[b * 512 + lane * 8 + i] = v[i]; y[b * 512 + lane * 8 + i] = yy[i]; }
}
__global__ void __launch_bounds__(256) k_w1(const float* y, const u16* w1, const u16* b1, float* h1) {
  __shared__ float red[32][8][8];
  const int tid = threadIdx.x, col = tid & 7, kp = tid >> 3;
  float acc[8] = {0};
  if (col < 7)
    for (int k = kp * 16; k < kp * 16 + 16; ++k) {
      float wv = bf2f(w1[(size_t)k * F + blockIdx.x * 7 + col]);
      for (int b = 0; b < 8; ++b) acc[b] += y[b * 512 + k] * wv;
    }
  for (int b = 0; b < 8; ++b) red[kp][b][col] = acc[b];
  __syncthreads();
  if (tid < 64) {
    int b = tid >> 3, c = tid & 7;
    if (c < 7) {
      float s = 0;
      for (int i = 0; i < 32; ++i) s += red[i][b][c];
      float z = rbf(rbf(s) + bf2f(b1[blockIdx.x * 7 + c]));
      float t = tanhf(0.7978845608028654f * (z + 0.044715f * z * z * z));
      h1[b * F + blockIdx.x * 7 + c] = rbf(0.5f * z * (1.f + t));
    }
  }
}
__global__ void __launch_bounds__(256) k_w2(const float* h1, const u16* w2, const u16* b2, const float* h, u16* out) {
  __shared__ float red[32][8][8];
  const int tid = threadIdx.x, col = tid & 7, kp = tid >> 3;
  float acc[8] = {0};
  for (int k = kp * 56; k < kp * 56 + 56; ++k) {
    float wv = bf2f(w2[(size_t)k * 512 + blockIdx.x * 8 + col]);
    for (int b = 0; b < 8; ++b) acc[b] += h1[b * F + k] * wv;
  }
  for (int b = 0; b < 8; ++b) red[kp][b][col] = acc[b];
  __syncthreads();
  if (tid < 64) {
    int b = tid >> 3, c = tid & 7;
    float s = 0;
    for (int i = 0; i < 32; ++i) s += red[i][b][c];
    int cc = blockIdx.x * 8 + c;
    float r = rbf(rbf(s) + bf2f(b2[cc]));
    out[b * 512 + cc] = (u16)(cvtpk(h[b * 512 + cc] + r, 0.f) & 0xffff);
  }
}

extern "C" void kernel_launch(void* const* d_in, const int* in_sizes, int n_in, void* d_out, int out_size, void* d_ws, size_t ws_size, hipStream_t stream) {
  const u16* x = (const u16*)d_in[0]; const u16* kc = (const u16*)d_in[1]; const u16* vc = (const u16*)d_in[2];
  const u16* wq = (const u16*)d_in[3]; const u16* wo = (const u16*)d_in[4];
  const u16* w1 = (const u16*)d_in[6]; const u16* b1 = (const u16*)d_in[7]; const u16* w2 = (const u16*)d_in[8]; const u16* b2 = (const u16*)d_in[9];
  float* ws = (float*)d_ws;
  float* q = ws; float* po = q + 8 * 4096; float* pml = po + 256 * 512; float* hp = pml + 256 * 8; float* h = hp + 32 * 8 * 512; float* y = h + 4096; float* h1 = y + 4096;
  k_q<<<256, 256, 0, stream>>>(x, wq, q);
  k_attn<<<256, 512, 0, stream>>>(kc, vc, q, po, pml);
  k_wo<<<256, 256, 0, stream>>>(po, pml, wo, hp);
  k_h<<<8, 64, 0, stream>>>(x, hp, h, y);
  k_w1<<<256, 256, 0, stream>>>(y, w1, b1, h1);
  k_w2<<<64, 256, 0, stream>>>(h1, w2, b2, h, (u16*)d_out);
}
```

```cpp
#include <hip/hip_runtime.h>
#include <stdint.h>

typedef unsigned v4u __attribute__((ext_vector_type(4)));
typedef unsigned v2u __attribute__((ext_vector_type(2)));
typedef float f32x4 __attribute__((ext_vector_type(4)));
typedef short bf16x8 __attribute__((ext_vector_type(8)));
typedef short s16x4 __attribute__((ext_vector_type(4)));
typedef unsigned short u16;
typedef unsigned long long u64;
#define LAS __attribute__((address_space(3)))

#define WIN 8
#define QDELAY 8
#define ADELAY 0
#define BDELAY 8
#define CDELAY 8
#define PSLEEP 2
#define KV_AFTER_S0 1

constexpr int D = 512, HKV = 8, SK = 4096, DH = 128, WW = 1024, F = 1792;
constexpr int WS_EPOCH = 0, WS_FLAGS = 1024, WS_Q = 4096, WS_BOX = 4096 + (1 << 20);
constexpr int BOX = 3456, BOX_ML = 1024, BOX_B = 1152, BOX_C = 1408;
constexpr int WS_STAMP = 4 << 20;
constexpr int L_WQ = 0;
constexpr int L_OP = 0;
constexpr int L_RED = 8192;
constexpr int L_RED2 = 12288;
constexpr int L_GS = 14336;
constexpr int L_WO = 16384;
constexpr int L_W1 = L_WO + 65536;
constexpr int L_W2 = L_W1 + 32768;
constexpr int L_SM = L_W2 + 32 * 1088;
constexpr int L_XN = L_SM;
constexpr int L_QS = L_XN + 8 * 272;
constexpr int L_SML = L_QS + 1024;
constexpr int L_ST = L_SML + 128;
constexpr int L_HS = L_ST + 64;
constexpr int L_YS = L_HS + 2048;
constexpr int L_CNT = L_YS + 2080;
constexpr int L_END = L_CNT + 64;
static_assert(L_END <= 160 * 1024, "lds");
static_assert((L_XN % 16) == 0 && (L_QS % 16) == 0 && (L_SML % 16) == 0 && (L_ST % 16) == 0 && (L_HS % 16) == 0 && (L_YS % 16) == 0 && (L_CNT % 16) == 0, "align");

constexpr unsigned SENT = 0xFFFFFFFFu;
constexpr int GSPIN = 20000;
constexpr int LSPIN = 1 << 22;

__device__ __forceinline__ unsigned f2u(float f) { return __builtin_bit_cast(unsigned, f); }
__device__ __forceinline__ float u2f(unsigned u) { return __builtin_bit_cast(float, u); }
__device__ __forceinline__ float bf2f(unsigned b) { return __builtin_bit_cast(float, b << 16); }
__device__ __forceinline__ float lo2f(unsigned d) { return __builtin_bit_cast(float, d << 16); }
__device__ __forceinline__ float hi2f(unsigned d) { return __builtin_bit_cast(float, d & 0xffff0000u); }
typedef __bf16 bf16x2 __attribute__((ext_vector_type(2)));
typedef float f32x2 __attribute__((ext_vector_type(2)));
__device__ __forceinline__ unsigned cvtpk(float lo, float hi) {
  return __builtin_bit_cast(unsigned, __builtin_convertvector(f32x2{lo, hi}, bf16x2));
}
__device__ __forceinline__ float rbf(float f) { return __builtin_bit_cast(float, cvtpk(f, f) << 16); }
__device__ __forceinline__ unsigned nosent(unsigned d) { return d < 0xFFFFFFFEu ? d : 0xFFFFFFFEu; }
__device__ __forceinline__ f32x4 mfma16(v4u a, v4u b, f32x4 c) {
  return __builtin_amdgcn_mfma_f32_16x16x32_bf16(__builtin_bit_cast(bf16x8, a), __builtin_bit_cast(bf16x8, b), c, 0, 0, 0);
}
template <int CTRL> __device__ __forceinline__ float dpp(float v) {
  return u2f(__builtin_amdgcn_update_dpp(0, __builtin_bit_cast(int, v), CTRL, 0xf, 0xf, true));
}
__device__ __forceinline__ void swap16(float& a, float& b) {
  auto p = __builtin_amdgcn_permlane16_swap(f2u(a), f2u(b), false, false);
  a = u2f(p[0]); b = u2f(p[1]);
}
__device__ __forceinline__ void swap32(float& a, float& b) {
  auto p = __builtin_amdgcn_permlane32_swap(f2u(a), f2u(b), false, false);
  a = u2f(p[0]); b = u2f(p[1]);
}
__device__ __forceinline__ float sum16(float v) { float a = v, b = v; swap16(a, b); return a + b; }
__device__ __forceinline__ float sum32(float v) { float a = v, b = v; swap32(a, b); return a + b; }
__device__ __forceinline__ float max16(float v) { float a = v, b = v; swap16(a, b); return fmaxf(a, b); }
__device__ __forceinline__ float max32(float v) { float a = v, b = v; swap32(a, b); return fmaxf(a, b); }
__device__ __forceinline__ float quad_sum(float v) { v += dpp<0xB1>(v); v += dpp<0x4E>(v); return v; }
__device__ __forceinline__ float quad_max(float v) { v = fmaxf(v, dpp<0xB1>(v)); v = fmaxf(v, dpp<0x4E>(v)); return v; }
__device__ __forceinline__ float wave_sum(float v) {
  v += dpp<0x128>(v); v += dpp<0x124>(v); v += dpp<0x122>(v); v += dpp<0x121>(v);
  return sum32(sum16(v));
}
__device__ __forceinline__ float fexp(float x) { return __builtin_amdgcn_exp2f(x * 1.4426950408889634f); }
__device__ __forceinline__ float frcp(float x) { return __builtin_amdgcn_rcpf(x); }

__device__ __forceinline__ void dma16(const void* g, unsigned ldsoff) {
  unsigned keep;
  asm volatile("s_mov_b32 %0, m0\n\ts_mov_b32 m0, %2\n\ts_nop 0\n\tglobal_load_lds_dwordx4 %1, off nt\n\ts_mov_b32 m0, %0"
               : "=&s"(keep) : "v"(g), "s"(ldsoff) : "memory");
}
#define WAITVM(n) asm volatile("s_waitcnt vmcnt(" #n ")" ::: "memory")
#define BAR() do { __builtin_amdgcn_sched_barrier(0); asm volatile("s_waitcnt lgkmcnt(0)\n\ts_barrier" ::: "memory"); __builtin_amdgcn_sched_barrier(0); } while (0)
#define RELOAD() asm volatile("" ::: "memory")
template <int N> __device__ __forceinline__ void waitvm() { asm volatile("s_waitcnt vmcnt(%0)" ::"n"(N) : "memory"); }
template <int N> __device__ __forceinline__ void nap() { for (int i = 0; i < N; ++i) __builtin_amdgcn_s_sleep(1); }

__device__ __forceinline__ v4u tr_frag(LAS char* sm, unsigned a0, unsigned a1) {
  s16x4 x = __builtin_amdgcn_ds_read_tr16_b64_v4i16((LAS s16x4*)(sm + a0));
  s16x4 y = __builtin_amdgcn_ds_read_tr16_b64_v4i16((LAS s16x4*)(sm + a1));
  v2u xx = __builtin_bit_cast(v2u, x), yy = __builtin_bit_cast(v2u, y);
  return v4u{xx.x, xx.y, yy.x, yy.y};
}
__device__ __forceinline__ unsigned swz128(unsigned row) { return (row & 2u) | ((row >> 1) & 4u); }

__device__ __forceinline__ void cnt_add(LAS char* sm, int i, int lane) {
  asm volatile("s_waitcnt lgkmcnt(0)" ::: "memory");
  if (lane == 0) __hip_atomic_fetch_add((LAS unsigned*)(sm + L_CNT) + i, 1u, __ATOMIC_RELAXED, __HIP_MEMORY_SCOPE_WORKGROUP);
  asm volatile("" ::: "memory");
}
__device__ __forceinline__ void cnt_wait(LAS char* sm, int i, unsigned n) {
#pragma unroll 1
  for (int it = 0; it < LSPIN; ++it) {
    unsigned v = __hip_atomic_load((LAS unsigned*)(sm + L_CNT) + i, __ATOMIC_RELAXED, __HIP_MEMORY_SCOPE_WORKGROUP);
    if (__builtin_amdgcn_readfirstlane(v) >= n) break;
    __builtin_amdgcn_s_sleep(1);
  }
  asm volatile("" ::: "memory");
}

#define STAMP(i) do {} while (0)

__global__ void __launch_bounds__(512, 2)
fused_block(const u16* __restrict__ x, const u16* __restrict__ kc, const u16* __restrict__ vc, const u16* __restrict__ wq,
            const u16* __restrict__ wo, const u16* __restrict__ w1, const u16* __restrict__ b1, const u16* __restrict__ w2,
            const u16* __restrict__ b2, u16* __restrict__ out, char* __restrict__ ws) {
  __shared__ __attribute__((aligned(1024))) char smem_[L_END];
  LAS char* sm = (LAS char*)smem_;
  const unsigned smb = (unsigned)(uintptr_t)sm;
  const int tid = threadIdx.x, lane = tid & 63, w = __builtin_amdgcn_readfirstlane(tid >> 6);
  const int c = blockIdx.x, g = c & 7, r = c >> 3;
  const int lg = lane >> 4, lc = lane & 15;
  const int tq = lc >> 2, tp = lane & 3;
  const __amdgpu_buffer_rsrc_t rs = __builtin_amdgcn_make_buffer_rsrc((void*)ws, 0, 0x7fffffff, 0x00020000);
  const int mybox = WS_BOX + c * BOX;
  const int bp = r & 3, jj = r >> 2, jt = 8 * jj + g;
  const bool mlp = jj < 7;
  STAMP(0);

  if (w < 4) {
    __builtin_amdgcn_s_setprio(3);
    const int kq = r >> 3, cb = r & 7;
    const unsigned E = __builtin_amdgcn_raw_buffer_load_b32(rs, 0, WS_EPOCH, 16);
    v4u xr[2];
#pragma unroll
    for (int i = 0; i < 2; ++i) xr[i] = *(const v4u*)(x + (2 * w + i) * D + lane * 8);
#pragma unroll
    for (int i = 0; i < 4; ++i) {
      const unsigned row = 32 * w + 8 * i + (lane >> 3);
      const unsigned ch = (lane & 7) ^ swz128(row);
      dma16(wq + (size_t)(128 * kq + row) * 4096 + 512 * g + 64 * cb + ch * 8, smb + L_WQ + (32 * w + 8 * i) * 128);
    }
    {
      const int idx = w * 64 + lane;
      if (idx < BOX / 16) __builtin_amdgcn_raw_buffer_store_b128(v4u{SENT, SENT, SENT, SENT}, rs, mybox + idx * 16, 0, 16);
    }
    if (tid < 8) *((LAS unsigned*)(sm + L_CNT) + tid) = 0;
    BAR();
    const unsigned tag = __builtin_amdgcn_readfirstlane(E) + 1u;
#pragma unroll
    for (int i = 0; i < 2; ++i) {
      float v[8];
#pragma unroll
      for (int k = 0; k < 4; ++k) { v[2 * k] = lo2f(xr[i][k]); v[2 * k + 1] = hi2f(xr[i][k]); }
      float s = 0.f;
#pragma unroll
      for (int k = 0; k < 8; ++k) s += v[k];
      const float mean = wave_sum(s) * (1.f / 512.f);
      float q2 = 0.f;
#pragma unroll
      for (int k = 0; k < 8; ++k) { const float d = v[k] - mean; q2 += d * d; }
      const float var = wave_sum(q2) * (1.f / 512.f);
      const float mb = rbf(mean);
      const float rden = frcp(rbf(__builtin_amdgcn_sqrtf(rbf(rbf(var) + 1e-5f))));
      v4u y;
#pragma unroll
      for (int k = 0; k < 4; ++k) y[k] = cvtpk(rbf(v[2 * k] - mb) * rden, rbf(v[2 * k + 1] - mb) * rden);
      if (lg == kq) *(LAS v4u*)(sm + L_XN + (2 * w + i) * 272 + lc * 16) = y;
    }
    WAITVM(0);
    STAMP(1);
    cnt_add(sm, 0, lane);
    cnt_wait(sm, 0, 4);
    if (tid == 0) __builtin_amdgcn_raw_buffer_store_b32(tag, rs, WS_FLAGS + c * 4, 0, 16);
    {
      f32x4 acc = {0, 0, 0, 0};
#pragma unroll
      for (int ks = 0; ks < 4; ++ks) {
        const unsigned r0 = 32 * ks + 8 * lg + tq, r1 = r0 + 4, ch = 2 * w + (tp >> 1);
        const v4u A = tr_frag(sm, L_WQ + r0 * 128 + ((ch ^ swz128(r0)) * 16) + 8 * (tp & 1), L_WQ + r1 * 128 + ((ch ^ swz128(r1)) * 16) + 8 * (tp & 1));
        const v4u Bf = *(LAS v4u*)(sm + L_XN + (lc & 7) * 272 + (32 * ks + 8 * lg) * 2);
        acc = mfma16(A, Bf, acc);
      }
      if (lc < 8) {
        const int off = WS_Q + ((((g * 8 + lc) * 4 + kq) * 512) + 64 * cb + 16 * w + 4 * lg) * 8;
        __builtin_amdgcn_raw_buffer_store_b128(v4u{f2u(acc[0]), tag, f2u(acc[1]), tag}, rs, off, 0, 16);
        __builtin_amdgcn_raw_buffer_store_b128(v4u{f2u(acc[2]), tag, f2u(acc[3]), tag}, rs, off + 16, 0, 16);
      }
    }
    STAMP(2);
    nap<QDELAY>();
    {
      const int b = r >> 2, ht = w * 64 + lane;
      const int off = WS_Q + ((g * 8 + b) * 4 * 512 + 2 * ht) * 8;
      v4u q[4];
      _Pragma("unroll 1") for (int it = 0; it < GSPIN; ++it) {
        RELOAD();
#pragma unroll
        for (int k = 0; k < 4; ++k) q[k] = __builtin_amdgcn_raw_buffer_load_b128(rs, off + k * 4096, 0, 16);
        bool ok = true;
#pragma unroll
        for (int k = 0; k < 4; ++k) ok &= (q[k].y == tag) & (q[k].w == tag);
        if (__all(ok)) break;
        nap<PSLEEP>();
      }
      float q0 = 0.f, q1 = 0.f;
#pragma unroll
      for (int k = 0; k < 4; ++k) { q0 += u2f(q[k].x); q1 += u2f(q[k].z); }
      *(LAS unsigned*)(sm + L_QS + ht * 4) = cvtpk(q0, q1);
      cnt_add(sm, 1, lane);
    }
    STAMP(3);
    if (w == 0) {
      _Pragma("unroll 1") for (int it = 0; it < GSPIN; ++it) {
        RELOAD();
        const v4u f = __builtin_amdgcn_raw_buffer_load_b128(rs, WS_FLAGS + lane * 16, 0, 16);
        if (__all((f.x == tag) & (f.y == tag) & (f.z == tag) & (f.w == tag))) break;
        nap<PSLEEP>();
      }
      if (tid == 0 && c == 0) __builtin_amdgcn_raw_buffer_store_b32(tag, rs, WS_EPOCH, 0, 16);
    }
    STAMP(4);
    cnt_wait(sm, 2, 4);
    __builtin_amdgcn_s_setprio(0);
    {
      const int t = r & 7;
#pragma unroll 4
      for (int i = 0; i < 16; ++i) {
        const unsigned row = 128 * w + 8 * i + (lane >> 3);
        const unsigned ch = (lane & 7) ^ swz128(row);
        dma16(wo + (size_t)(512 * g + row) * 512 + 64 * t + ch * 8, smb + L_WO + (128 * w + 8 * i) * 128);
      }
      if (mlp) {
#pragma unroll 4
        for (int i = 0; i < 8; ++i) {
          const unsigned row = 128 * w + 16 * i + (lane >> 2);
          const unsigned ch = (lane & 3) ^ ((row >> 2) & 2);
          dma16(w1 + (size_t)row * F + 32 * jt + ch * 8, smb + L_W1 + (128 * w + 16 * i) * 64);
        }
#pragma unroll 4
        for (int i = 0; i < 8; ++i) {
          const unsigned row = 8 * w + i;
          dma16(w2 + (size_t)(32 * jt + row) * 512 + lane * 8, smb + L_W2 + row * 1088);
        }
        waitvm<16>();
      } else {
        waitvm<0>();
      }
    }
    STAMP(5);
    __builtin_amdgcn_s_setprio(3);
  } else {
    const int kw = w - 4, b = r >> 2, s = r & 3;
    const size_t base = ((size_t)(b * HKV + g) * SK + (SK - WW) + s * 256 + kw * 64) * DH;
    const u16* kp = kc + base + (size_t)(lc >> 2) * DH + (4 * lg + (lc & 3)) * 8;
    const u16* vp = vc + base + (size_t)lg * DH + lc * 8;
    v4u Kr[16], Vr[16];
    if (KV_AFTER_S0) BAR();
    __builtin_amdgcn_s_setprio(1);
#pragma unroll
    for (int i = 0; i < 32; ++i) {
      if (i == WIN && !KV_AFTER_S0) BAR();
      if (i >= WIN) waitvm<(WIN - 1 < 63 ? WIN - 1 : 63)>();
      if (i < 16) Kr[i] = __builtin_nontemporal_load((const v4u*)(kp + (size_t)i * 4 * DH));
      else Vr[i - 16] = __builtin_nontemporal_load((const v4u*)(vp + (size_t)(i - 16) * 4 * DH));
      __builtin_amdgcn_sched_barrier(0);
    }
    cnt_add(sm, 2, lane);
    STAMP(1);
    cnt_wait(sm, 1, 4);
    STAMP(2);
    const v4u Qf = *(LAS v4u*)(sm + L_QS + ((lc >> 2) * 128 + (4 * lg + (lc & 3)) * 8) * 2);
    float sc[16];
    const int e = lc & 3;
#pragma unroll
    for (int i = 0; i < 16; ++i) {
      const f32x4 d = mfma16(Kr[i], Qf, f32x4{0, 0, 0, 0});
      float v = e == 0 ? d[0] : e == 1 ? d[1] : e == 2 ? d[2] : d[3];
      v = quad_sum(v);
      sc[i] = rbf(rbf(v) * 0.08838834764831845f);
    }
    float m = sc[0];
#pragma unroll
    for (int i = 1; i < 16; ++i) m = fmaxf(m, sc[i]);
    m = max32(max16(m));
    float l = 0.f;
#pragma unroll
    for (int i = 0; i < 16; ++i) { sc[i] = fexp(sc[i] - m); l += sc[i]; }
    l = sum32(sum16(l));
    v4u Pf[2];
#pragma unroll
    for (int hh = 0; hh < 2; ++hh)
#pragma unroll
      for (int k = 0; k < 4; ++k) Pf[hh][k] = cvtpk(sc[8 * hh + 2 * k], sc[8 * hh + 2 * k + 1]);
    if (lg == 0 && e == 0) *(LAS v2u*)(sm + L_SML + (kw * 4 + (lc >> 2)) * 8) = v2u{f2u(m), f2u(l)};
    STAMP(3);
    f32x4 o[8];
#pragma unroll
    for (int hh = 0; hh < 2; ++hh) {
#pragma unroll
      for (int k = 0; k < 8; ++k) {
        const unsigned sel = (k & 1) ? 0x07060302u : 0x05040100u;
        const int dw = k >> 1;
        v4u Bf;
        Bf.x = __builtin_amdgcn_perm(Vr[8 * hh + 1][dw], Vr[8 * hh + 0][dw], sel);
        Bf.y = __builtin_amdgcn_perm(Vr[8 * hh + 3][dw], Vr[8 * hh + 2][dw], sel);
        Bf.z = __builtin_amdgcn_perm(Vr[8 * hh + 5][dw], Vr[8 * hh + 4][dw], sel);
        Bf.w = __builtin_amdgcn_perm(Vr[8 * hh + 7][dw], Vr[8 * hh + 6][dw], sel);
        o[k] = mfma16(Pf[hh], Bf, hh == 0 ? f32x4{0, 0, 0, 0} : o[k]);
      }
    }
    STAMP(4);
    *(LAS f32x4*)(sm + L_OP + kw * 2048 + (lg * 128 + lc * 8) * 4) = f32x4{o[0][0], o[1][0], o[2][0], o[3][0]};
    *(LAS f32x4*)(sm + L_OP + kw * 2048 + (lg * 128 + lc * 8) * 4 + 16) = f32x4{o[4][0], o[5][0], o[6][0], o[7][0]};
    cnt_add(sm, 3, lane);
    cnt_wait(sm, 3, 4);
    {
      float mm[4], ll[4];
#pragma unroll
      for (int k = 0; k < 4; ++k) {
        const v2u t = *(LAS v2u*)(sm + L_SML + (k * 4 + kw) * 8);
        mm[k] = u2f(t.x); ll[k] = u2f(t.y);
      }
      const float M = fmaxf(fmaxf(mm[0], mm[1]), fmaxf(mm[2], mm[3]));
      float L = 0.f, a0 = 0.f, a1 = 0.f;
#pragma unroll
      for (int k = 0; k < 4; ++k) {
        const float f = fexp(mm[k] - M);
        const v2u t = *(LAS v2u*)(sm + L_OP + k * 2048 + (kw * 128 + 2 * lane) * 4);
        L += f * ll[k]; a0 += f * u2f(t.x); a1 += f * u2f(t.y);
      }
      const float rl = frcp(L);
      __builtin_amdgcn_raw_buffer_store_b32(nosent(cvtpk(a0 * rl, a1 * rl)), rs, mybox + kw * 256 + lane * 4, 0, 16);
      if (lane == 0) __builtin_amdgcn_raw_buffer_store_b64(v2u{nosent(f2u(M)), nosent(f2u(L))}, rs, mybox + BOX_ML + kw * 8, 0, 16);
    }
    STAMP(5);
    __builtin_amdgcn_s_setprio(3);
  }

  const int hb = tid >> 8, cp = tid & 255;
  const int uu = 8 * jj + g, fb = uu >> 5, fcol = 16 * (uu & 31) + 8 * (lane & 1) + 2 * ((lane >> 4) & 1) + 4 * (lane >> 5);
  const unsigned xpair = *(const unsigned*)(x + (2 * bp + hb) * D + 2 * cp);
  const unsigned b1v = b1[(mlp ? 32 * jt : 0) + (lane & 31)];
  const unsigned b2pair = *(const unsigned*)(b2 + fcol);
  BAR();
  {
    const int hl = w >> 1, hf = w & 1, bpA = r >> 3;
    v4u Aw[4][2];
#pragma unroll
    for (int ks = 0; ks < 2; ++ks)
#pragma unroll
      for (int ct = 0; ct < 4; ++ct) {
        const unsigned r0 = hl * 128 + hf * 64 + 32 * ks + 8 * lg + tq, r1 = r0 + 4, ch = 2 * ct + (tp >> 1);
        Aw[ct][ks] = tr_frag(sm, L_WO + r0 * 128 + ((ch ^ swz128(r0)) * 16) + 8 * (tp & 1), L_WO + r1 * 128 + ((ch ^ swz128(r1)) * 16) + 8 * (tp & 1));
      }
    nap<ADELAY>();
    STAMP(6);
    const int pbox = WS_BOX + (g + 8 * (8 * bpA + (lc & 7))) * BOX;
    const int offo = pbox + (hl * 128 + hf * 64 + 8 * lg) * 2;
    v4u Bo[2]; v2u ml;
    _Pragma("unroll 1") for (int it = 0; it < GSPIN; ++it) {
        RELOAD();
      Bo[0] = __builtin_amdgcn_raw_buffer_load_b128(rs, offo, 0, 16);
      Bo[1] = __builtin_amdgcn_raw_buffer_load_b128(rs, offo + 64, 0, 16);
      ml = __builtin_amdgcn_raw_buffer_load_b64(rs, pbox + BOX_ML + hl * 8, 0, 16);
      bool ok = (ml.x != SENT) & (ml.y != SENT);
#pragma unroll
      for (int k = 0; k < 4; ++k) ok &= (Bo[0][k] != SENT) & (Bo[1][k] != SENT);
      if (__all(ok)) break;
      nap<PSLEEP>();
    }
    STAMP(7);
    const float m_ = u2f(ml.x), l_ = u2f(ml.y);
    const float wgt = l_ * fexp(m_ - quad_max(m_));
    const float f = wgt * frcp(quad_sum(wgt));
#pragma unroll
    for (int ct = 0; ct < 4; ++ct) {
      f32x4 acc = mfma16(Aw[ct][0], Bo[0], f32x4{0, 0, 0, 0});
      acc = mfma16(Aw[ct][1], Bo[1], acc);
#pragma unroll
      for (int k = 0; k < 4; ++k) acc[k] = quad_sum(acc[k] * f);
      if ((lc & 11) == 0) *(LAS f32x4*)(sm + L_RED + w * 512 + (lc >> 2) * 256 + (16 * ct + 4 * lg) * 4) = acc;
    }
  }
  WAITVM(0);
  BAR();
  if (w == 0) {
    float a0 = 0.f, a1 = 0.f;
#pragma unroll
    for (int k = 0; k < 8; ++k) {
      const v2u t = *(LAS v2u*)(sm + L_RED + k * 512 + (lane >> 5) * 256 + (lane & 31) * 8);
      a0 += u2f(t.x); a1 += u2f(t.y);
    }
    __builtin_amdgcn_raw_buffer_store_b32(nosent(cvtpk(a0, a1)), rs, mybox + BOX_B + (lane >> 5) * 128 + (lane & 31) * 4, 0, 16);
  }
  STAMP(8);
  v4u A1[2][2], A2[4];
  if (mlp) {
#pragma unroll
    for (int ks = 0; ks < 2; ++ks)
#pragma unroll
      for (int ct = 0; ct < 2; ++ct) {
        const unsigned r0 = 64 * w + 32 * ks + 8 * lg + tq, r1 = r0 + 4, ch = 2 * ct + (tp >> 1);
        A1[ct][ks] = tr_frag(sm, L_W1 + r0 * 64 + ((ch ^ ((r0 >> 2) & 2)) * 16) + 8 * (tp & 1), L_W1 + r1 * 64 + ((ch ^ ((r1 >> 2) & 2)) * 16) + 8 * (tp & 1));
      }
#pragma unroll
    for (int ct = 0; ct < 4; ++ct) {
      const unsigned r0 = 8 * lg + tq, r1 = r0 + 4;
      const unsigned co = (8 * w + 4 * (ct >> 1) + tp) * 16 + 8 * (ct & 1);
      A2[ct] = tr_frag(sm, L_W2 + r0 * 1088 + co, L_W2 + r1 * 1088 + co);
    }
  }
  nap<BDELAY>();
  float h0, h1;
  {
    const int off = WS_BOX + (8 * (8 * bp + (cp >> 5))) * BOX + BOX_B + hb * 128 + (cp & 31) * 4;
    unsigned p[8];
    _Pragma("unroll 1") for (int it = 0; it < GSPIN; ++it) {
        RELOAD();
#pragma unroll
      for (int k = 0; k < 8; ++k) p[k] = __builtin_amdgcn_raw_buffer_load_b32(rs, off + k * BOX, 0, 16);
      bool ok = true;
#pragma unroll
      for (int k = 0; k < 8; ++k) ok &= (p[k] != SENT);
      if (__all(ok)) break;
      nap<PSLEEP>();
    }
    STAMP(9);
    float a0 = 0.f, a1 = 0.f;
#pragma unroll
    for (int k = 0; k < 8; ++k) { a0 += lo2f(p[k]); a1 += hi2f(p[k]); }
    const unsigned hp = cvtpk(lo2f(xpair) + rbf(a0), hi2f(xpair) + rbf(a1));
    h0 = lo2f(hp); h1 = hi2f(hp);
    *(LAS unsigned*)(sm + L_HS + hb * 1024 + cp * 4) = hp;
  }
  if (mlp) {
    const float s = wave_sum(h0 + h1);
    const float mw = s * (1.f / 128.f);
    const float m2 = wave_sum((h0 - mw) * (h0 - mw) + (h1 - mw) * (h1 - mw));
    if (lane == 0) *(LAS v2u*)(sm + L_ST + w * 8) = v2u{f2u(s), f2u(m2)};
  }
  BAR();
  if (mlp) {
    {
      const f32x4 s01 = *(LAS f32x4*)(sm + L_ST + hb * 32), s23 = *(LAS f32x4*)(sm + L_ST + hb * 32 + 16);
      const float mean = (s01[0] + s01[2] + s23[0] + s23[2]) * (1.f / 512.f);
      const float d0 = s01[0] * (1.f / 128.f) - mean, d1 = s01[2] * (1.f / 128.f) - mean, d2 = s23[0] * (1.f / 128.f) - mean, d3 = s23[2] * (1.f / 128.f) - mean;
      const float var = (s01[1] + s01[3] + s23[1] + s23[3] + 128.f * (d0 * d0 + d1 * d1 + d2 * d2 + d3 * d3)) * (1.f / 512.f);
      const float mb = rbf(mean);
      const float rden = frcp(rbf(__builtin_amdgcn_sqrtf(rbf(rbf(var) + 1e-5f))));
      *(LAS unsigned*)(sm + L_YS + hb * 1040 + cp * 4) = cvtpk(rbf(h0 - mb) * rden, rbf(h1 - mb) * rden);
    }
    BAR();
    {
      f32x4 acc[2] = {{0, 0, 0, 0}, {0, 0, 0, 0}};
#pragma unroll
      for (int ks = 0; ks < 2; ++ks) {
        const v4u Bf = *(LAS v4u*)(sm + L_YS + (lc & 1) * 1040 + (64 * w + 32 * ks + 8 * lg) * 2);
        acc[0] = mfma16(A1[0][ks], Bf, acc[0]);
        acc[1] = mfma16(A1[1][ks], Bf, acc[1]);
      }
      if (lc < 2) {
        *(LAS f32x4*)(sm + L_RED2 + w * 256 + lc * 128 + (4 * lg) * 4) = acc[0];
        *(LAS f32x4*)(sm + L_RED2 + w * 256 + lc * 128 + (16 + 4 * lg) * 4) = acc[1];
      }
    }
    BAR();
    {
      float z = 0.f;
#pragma unroll
      for (int k = 0; k < 8; ++k) z += *(LAS float*)(sm + L_RED2 + k * 256 + lane * 4);
      z = rbf(rbf(z) + bf2f(b1v));
      const float u = 0.7978845608028654f * (z + 0.044715f * z * z * z);
      const float th = 1.f - 2.f * frcp(fexp(2.f * u) + 1.f);
      const float gl = 0.5f * z * (1.f + th);
      *(LAS u16*)(sm + L_GS + w * 128 + lane * 2) = (u16)(cvtpk(gl, gl) & 0xffffu);
      asm volatile("s_waitcnt lgkmcnt(0)" ::: "memory");
      const v4u Bf = *(LAS v4u*)(sm + L_GS + w * 128 + (lc & 1) * 64 + lg * 16);
      f32x4 o[4];
#pragma unroll
      for (int ct = 0; ct < 4; ++ct) o[ct] = mfma16(A2[ct], Bf, f32x4{0, 0, 0, 0});
      if (lc < 2) {
#pragma unroll
        for (int hh = 0; hh < 2; ++hh) {
          const v4u t = {nosent(cvtpk(o[2 * hh][0], o[2 * hh][1])), nosent(cvtpk(o[2 * hh][2], o[2 * hh][3])),
                         nosent(cvtpk(o[2 * hh + 1][0], o[2 * hh + 1][1])), nosent(cvtpk(o[2 * hh + 1][2], o[2 * hh + 1][3]))};
          __builtin_amdgcn_raw_buffer_store_b128(t, rs, mybox + BOX_C + lc * 1024 + (64 * w + 32 * hh + 8 * lg) * 2, 0, 16);
        }
      }
    }
  }
  STAMP(10);
  if (w == 0) {
    nap<CDELAY>();
    const int pj = lane >> 1;
    const int j0 = pj, j1 = pj < 24 ? 32 + pj : pj;
    const int o0 = WS_BOX + ((j0 & 7) + 8 * (4 * (j0 >> 3) + bp)) * BOX + BOX_C + fb * 1024 + (16 * (uu & 31) + 8 * (lane & 1)) * 2;
    const int o1 = WS_BOX + ((j1 & 7) + 8 * (4 * (j1 >> 3) + bp)) * BOX + BOX_C + fb * 1024 + (16 * (uu & 31) + 8 * (lane & 1)) * 2;
    v4u p0, p1;
    _Pragma("unroll 1") for (int it = 0; it < GSPIN; ++it) {
        RELOAD();
      p0 = __builtin_amdgcn_raw_buffer_load_b128(rs, o0, 0, 16);
      p1 = __builtin_amdgcn_raw_buffer_load_b128(rs, o1, 0, 16);
      bool ok = true;
#pragma unroll
      for (int k = 0; k < 4; ++k) ok &= (p0[k] != SENT) & (p1[k] != SENT);
      if (__all(ok)) break;
      nap<PSLEEP>();
    }
    STAMP(11);
    const float k1 = pj < 24 ? 1.f : 0.f;
    float v[8];
#pragma unroll
    for (int k = 0; k < 4; ++k) {
      v[2 * k] = lo2f(p0[k]) + (pj < 24 ? lo2f(p1[k]) : 0.f);
      v[2 * k + 1] = hi2f(p0[k]) + (pj < 24 ? hi2f(p1[k]) : 0.f);
    }
    (void)k1;
    float u4[4];
#pragma unroll
    for (int k = 0; k < 4; ++k) { float a = v[k], bb = v[k + 4]; swap32(a, bb); u4[k] = a + bb; }
    float u2[2];
#pragma unroll
    for (int k = 0; k < 2; ++k) { float a = u4[k], bb = u4[k + 2]; swap16(a, bb); u2[k] = a + bb; }
#pragma unroll
    for (int k = 0; k < 2; ++k) { u2[k] += dpp<0x128>(u2[k]); u2[k] += dpp<0x124>(u2[k]); u2[k] += dpp<0x122>(u2[k]); }
    const unsigned hp = *(LAS unsigned*)(sm + L_HS + fb * 1024 + fcol * 2);
    const float r0 = rbf(rbf(u2[0]) + lo2f(b2pair)), r1 = rbf(rbf(u2[1]) + hi2f(b2pair));
    if ((lane & 14) == 0) *(unsigned*)(out + (2 * bp + fb) * D + fcol) = cvtpk(lo2f(hp) + r0, hi2f(hp) + r1);
  }
  STAMP(12);
}

extern "C" void kernel_launch(void* const* d_in, const int* in_sizes, int n_in, void* d_out, int out_size, void* d_ws,
                              size_t ws_size, hipStream_t stream) {
  fused_block<<<256, 512, 0, stream>>>((const u16*)d_in[0], (const u16*)d_in[1], (const u16*)d_in[2], (const u16*)d_in[3],
                                       (const u16*)d_in[4], (const u16*)d_in[6], (const u16*)d_in[7], (const u16*)d_in[8],
                                       (const u16*)d_in[9], (u16*)d_out, (char*)d_ws);
}
```

```cpp
#include <hip/hip_runtime.h>
#include <stdint.h>

typedef unsigned v4u __attribute__((ext_vector_type(4)));
typedef unsigned v2u __attribute__((ext_vector_type(2)));
typedef float f32x4 __attribute__((ext_vector_type(4)));
typedef short bf16x8 __attribute__((ext_vector_type(8)));
typedef short s16x4 __attribute__((ext_vector_type(4)));
typedef unsigned short u16;
typedef unsigned long long u64;
#define LAS __attribute__((address_space(3)))

constexpr int W0 = 4;
constexpr int W1 = 4;
constexpr int QDELAY = 32;
constexpr int ADELAY = 10;
constexpr int BDELAY = 8;
constexpr int CDELAY = 62;
constexpr int PSLEEP = 2;
#define CPF 4
#define CPF_BYTES 12800

constexpr int D = 512, HKV = 8, SK = 4096, DH = 128, WW = 1024, F = 1792;
constexpr int WS_EPOCH = 0, WS_Q = 4096, WS_FLAGS = 4096 + (1 << 20), WS_BOX = WS_FLAGS + 256 * 128;
constexpr int BOX = 3840, BOX_ML = 1024, BOX_B = 1536, BOX_C = 1792;
constexpr int L_WQ = 0;
constexpr int L_OP = 0;
constexpr int L_WO = 16384;
constexpr int L_W1 = L_WO + 32768;
constexpr int L_W2 = L_W1 + 32768;
constexpr int L_XN = L_W2 + 32768;
constexpr int L_QS = L_XN + 8 * 272;
constexpr int L_SM = L_QS + 1024;
constexpr int L_SL = L_SM + 128;
constexpr int L_ST = L_SL + 128;
constexpr int L_HS = L_ST + 128;
constexpr int L_YS = L_HS + 2048;
constexpr int L_RED = L_YS + 2080;
constexpr int L_RED2 = L_RED + 4096;
constexpr int L_GS = L_RED2 + 2048;
constexpr int L_CNT = L_GS + 1024;
constexpr int L_END = L_CNT + 64;

constexpr unsigned SENT = 0xFFFFFFFFu;
constexpr int GSPIN = 20000;
constexpr int LSPIN = 1 << 22;

__device__ __forceinline__ unsigned f2u(float f) { return __builtin_bit_cast(unsigned, f); }
__device__ __forceinline__ float u2f(unsigned u) { return __builtin_bit_cast(float, u); }
__device__ __forceinline__ float bf2f(unsigned b) { return __builtin_bit_cast(float, b << 16); }
__device__ __forceinline__ float lo2f(unsigned d) { return __builtin_bit_cast(float, d << 16); }
__device__ __forceinline__ float hi2f(unsigned d) { return __builtin_bit_cast(float, d & 0xffff0000u); }
typedef __bf16 bf16x2 __attribute__((ext_vector_type(2)));
typedef float f32x2 __attribute__((ext_vector_type(2)));
__device__ __forceinline__ unsigned cvtpk(float lo, float hi) {
  return __builtin_bit_cast(unsigned, __builtin_convertvector(f32x2{lo, hi}, bf16x2));
}
__device__ __forceinline__ float rbf(float f) { return __builtin_bit_cast(float, cvtpk(f, f) << 16); }
__device__ __forceinline__ unsigned nosent(unsigned d) { return d < 0xFFFFFFFEu ? d : 0xFFFFFFFEu; }
__device__ __forceinline__ f32x4 mfma16(v4u a, v4u b, f32x4 c) {
  return __builtin_amdgcn_mfma_f32_16x16x32_bf16(__builtin_bit_cast(bf16x8, a), __builtin_bit_cast(bf16x8, b), c, 0, 0, 0);
}
template <int CTRL> __device__ __forceinline__ float dpp(float v) {
  return u2f(__builtin_amdgcn_update_dpp(0, __builtin_bit_cast(int, v), CTRL, 0xf, 0xf, true));
}
__device__ __forceinline__ void swap16(float& a, float& b) {
  auto p = __builtin_amdgcn_permlane16_swap(f2u(a), f2u(b), false, false);
  a = u2f(p[0]); b = u2f(p[1]);
}
__device__ __forceinline__ void swap32(float& a, float& b) {
  auto p = __builtin_amdgcn_permlane32_swap(f2u(a), f2u(b), false, false);
  a = u2f(p[0]); b = u2f(p[1]);
}
__device__ __forceinline__ float sum16(float v) { float a = v, b = v; swap16(a, b); return a + b; }
__device__ __forceinline__ float sum32(float v) { float a = v, b = v; swap32(a, b); return a + b; }
__device__ __forceinline__ float max16(float v) { float a = v, b = v; swap16(a, b); return fmaxf(a, b); }
__device__ __forceinline__ float max32(float v) { float a = v, b = v; swap32(a, b); return fmaxf(a, b); }
__device__ __forceinline__ float quad_sum(float v) { v += dpp<0xB1>(v); v += dpp<0x4E>(v); return v; }
__device__ __forceinline__ float quad_max(float v) { v = fmaxf(v, dpp<0xB1>(v)); v = fmaxf(v, dpp<0x4E>(v)); return v; }
__device__ __forceinline__ float wave_sum(float v) {
  v += dpp<0x128>(v); v += dpp<0x124>(v); v += dpp<0x122>(v); v += dpp<0x121>(v);
  return sum32(sum16(v));
}
__device__ __forceinline__ float half_sum(float v) {
  v += dpp<0x128>(v); v += dpp<0x124>(v); v += dpp<0x122>(v); v += dpp<0x121>(v);
  return sum16(v);
}
__device__ __forceinline__ float fexp(float x) { return __builtin_amdgcn_exp2f(x * 1.4426950408889634f); }
__device__ __forceinline__ float frcp(float x) { return __builtin_amdgcn_rcpf(x); }

__device__ __forceinline__ void dma16s(unsigned voff, const void* sbase, unsigned ldsoff) {
  unsigned keep;
  asm volatile("s_mov_b32 %0, m0\n\ts_mov_b32 m0, %3\n\ts_nop 0\n\tglobal_load_lds_dwordx4 %1, %2 offset:0 nt\n\ts_mov_b32 m0, %0"
               : "=&s"(keep) : "v"(voff), "s"(sbase), "s"(ldsoff) : "memory");
}
__device__ __forceinline__ void dma16w(unsigned voff, const void* sbase, unsigned ldsoff) {
  unsigned keep;
  asm volatile("s_mov_b32 %0, m0\n\ts_mov_b32 m0, %3\n\ts_nop 0\n\tglobal_load_lds_dwordx4 %1, %2 offset:0\n\ts_mov_b32 m0, %0"
               : "=&s"(keep) : "v"(voff), "s"(sbase), "s"(ldsoff) : "memory");
}
#define WAITVM(n) asm volatile("s_waitcnt vmcnt(" #n ")" ::: "memory")
#define BAR() do { __builtin_amdgcn_sched_barrier(0); asm volatile("s_waitcnt lgkmcnt(0)\n\ts_barrier" ::: "memory"); __builtin_amdgcn_sched_barrier(0); } while (0)
#define RELOAD() asm volatile("" ::: "memory")
template <int N> __device__ __forceinline__ void waitvm() { asm volatile("s_waitcnt vmcnt(%0)" ::"n"(N) : "memory"); }
template <int N> __device__ __forceinline__ void nap() { if (N > 0) __builtin_amdgcn_s_sleep(N); }

__device__ __forceinline__ v4u tr_frag(LAS char* sm, unsigned a0, unsigned a1) {
  s16x4 x = __builtin_amdgcn_ds_read_tr16_b64_v4i16((LAS s16x4*)(sm + a0));
  s16x4 y = __builtin_amdgcn_ds_read_tr16_b64_v4i16((LAS s16x4*)(sm + a1));
  v2u xx = __builtin_bit_cast(v2u, x), yy = __builtin_bit_cast(v2u, y);
  return v4u{xx.x, xx.y, yy.x, yy.y};
}
__device__ __forceinline__ unsigned swz128(unsigned row) { return (row & 2u) | ((row >> 1) & 4u); }

__device__ __forceinline__ void cnt_add(LAS char* sm, int i, int lane) {
  asm volatile("s_waitcnt lgkmcnt(0)" ::: "memory");
  if (lane == 0) __hip_atomic_fetch_add((LAS unsigned*)(sm + L_CNT) + i, 1u, __ATOMIC_RELAXED, __HIP_MEMORY_SCOPE_WORKGROUP);
  asm volatile("" ::: "memory");
}
__device__ __forceinline__ void cnt_wait(LAS char* sm, int i, unsigned n) {
#pragma unroll 1
  for (int it = 0; it < LSPIN; ++it) {
    unsigned v = __hip_atomic_load((LAS unsigned*)(sm + L_CNT) + i, __ATOMIC_RELAXED, __HIP_MEMORY_SCOPE_WORKGROUP);
    if (__builtin_amdgcn_readfirstlane(v) >= n) break;
    __builtin_amdgcn_s_sleep(1);
  }
  asm volatile("" ::: "memory");
}


__device__ __forceinline__ f32x4 mfma16k16(unsigned a0, unsigned a1, unsigned b0, unsigned b1, f32x4 c) {
  return __builtin_amdgcn_mfma_f32_16x16x16bf16_1k(__builtin_bit_cast(s16x4, v2u{a0, a1}), __builtin_bit_cast(s16x4, v2u{b0, b1}), c, 0, 0, 0);
}
template <int IMM> __device__ __forceinline__ void gld_nt(v4u& dst, unsigned voff, const void* sbase) {
  asm volatile("global_load_dwordx4 %0, %1, %2 offset:%3 nt" : "=v"(dst) : "v"(voff), "s"(sbase), "n"(IMM) : "memory");
}
template <int N> __device__ __forceinline__ void waitvm_tie(v4u& r) { asm volatile("s_waitcnt vmcnt(%1)" : "+v"(r) : "n"(N) : "memory"); }

__global__ void __launch_bounds__(768, 3)
fused_block(const u16* __restrict__ x, const u16* __restrict__ kc, const u16* __restrict__ vc, const u16* __restrict__ wq,
            const u16* __restrict__ wo, const u16* __restrict__ w1, const u16* __restrict__ b1, const u16* __restrict__ w2,
            const u16* __restrict__ b2, u16* __restrict__ out, char* __restrict__ ws) {
  __shared__ __attribute__((aligned(1024))) char smem_[L_END];
  LAS char* sm = (LAS char*)smem_;
  const unsigned smb = (unsigned)(uintptr_t)sm;
  const int tid = threadIdx.x, lane = tid & 63, w = __builtin_amdgcn_readfirstlane(tid >> 6);
  const int c = blockIdx.x, g = c & 7, r = c >> 3;
  const int lg = lane >> 4, lc = lane & 15;
  const int tq = lc >> 2, tp = lane & 3;
  const __amdgpu_buffer_rsrc_t rs = __builtin_amdgcn_make_buffer_rsrc((void*)ws, 0, 0x7fffffff, 0x00020000);
  const int mybox = WS_BOX + c * BOX;
  if (w >= 10 && r < CPF) {
    u64 pc;
    asm volatile("s_getpc_b64 %0" : "=s"(pc));
    pc &= ~(u64)127;
    const unsigned vo = (w - 10) * 64 * 128 + lane * 128;
    if (vo < CPF_BYTES) {
      unsigned keep;
      asm volatile("s_mov_b32 %0, m0\n\ts_mov_b32 m0, %3\n\ts_nop 0\n\tglobal_load_lds_dword %1, %2 offset:0\n\ts_mov_b32 m0, %0"
                   : "=&s"(keep) : "v"(vo), "s"(pc), "s"(smb + L_RED + (w - 10) * 256) : "memory");
    }
  }

  if (w >= 4) {
  const int kw = w - 4;
  const int bp = r & 3, jj = r >> 2, jt = 8 * jj + g;
  const bool mlp = jj < 7;
  const int hb = lane >> 5, cp = 32 * kw + (lane & 31);
  const int fb = kw & 1, fcol = 64 * g + 8 * (lane & 7) + 2 * ((lane >> 4) & 1) + 4 * (lane >> 5);
  const unsigned xpair = *(const unsigned*)(x + (2 * bp + hb) * D + 2 * cp);
  const unsigned b1v = b1[(mlp ? 32 * jt : 0) + (lane & 31)];
  const unsigned b2pair = *(const unsigned*)(b2 + fcol);
  const __amdgpu_buffer_rsrc_t ro = __builtin_amdgcn_make_buffer_rsrc((void*)out, 0, 8192, 0x00020000);
  const int ooff = ((2 * bp + fb) * D + fcol) * 2;
  unsigned owarm = 0;
  if (!mlp && kw < 2) owarm = __builtin_amdgcn_raw_buffer_load_b32(ro, ooff, 0, 16);
  RELOAD();
  f32x4 oa0 = {0, 0, 0, 0}, oa1 = {0, 0, 0, 0};
  float Mh;
  {
    const int b = r >> 2, s = r & 3;
    const size_t base = ((size_t)(b * HKV + g) * SK + (SK - WW) + s * 256 + kw * 32) * DH + 2048;
    const u16* kb = kc + base;
    const u16* vb = vc + base;
    const unsigned ko = (lc >> 2) * 256 + (4 * lg + (lc & 3)) * 16, vo = lane * 16;
    v4u Kr[8], Vr[8];
    BAR();
#define KVLD(n) do { if ((n) < 8) gld_nt<((n) & 7) * 1024 - 4096>(Kr[(n) & 7], ko, kb); else gld_nt<((n) & 7) * 1024 - 4096>(Vr[(n) & 7], vo, vb); } while (0)
#define KVRANGE(a, b) do { \
      if (0 >= (a) && 0 < (b)) KVLD(0); if (1 >= (a) && 1 < (b)) KVLD(1); if (2 >= (a) && 2 < (b)) KVLD(2); if (3 >= (a) && 3 < (b)) KVLD(3); \
      if (4 >= (a) && 4 < (b)) KVLD(4); if (5 >= (a) && 5 < (b)) KVLD(5); if (6 >= (a) && 6 < (b)) KVLD(6); if (7 >= (a) && 7 < (b)) KVLD(7); \
      if (8 >= (a) && 8 < (b)) KVLD(8); if (9 >= (a) && 9 < (b)) KVLD(9); if (10 >= (a) && 10 < (b)) KVLD(10); if (11 >= (a) && 11 < (b)) KVLD(11); \
      if (12 >= (a) && 12 < (b)) KVLD(12); if (13 >= (a) && 13 < (b)) KVLD(13); if (14 >= (a) && 14 < (b)) KVLD(14); if (15 >= (a) && 15 < (b)) KVLD(15); } while (0)
    const unsigned vo128 = (lane >> 3) * 1024 + (((lane & 7) ^ ((lane >> 3) & 2)) * 16);
    const int jt_ = (r >> 2) < 7 ? 8 * (r >> 2) + g : g;
    const unsigned vo1 = (lane >> 2) * 3584 + (((lane & 3) ^ ((lane >> 4) & 2)) * 16);
    const unsigned vow = (lane >> 2) * 1024 + (((lane & 3) ^ ((lane >> 4) & 2)) * 16);
#define WO_LD(i) dma16w(vow, wo + (size_t)(512 * g + 64 * kw + 16 * (i)) * 512 + 32 * (r & 15), smb + L_WO + kw * 4096 + (i) * 1024)
#define W12_LD(i) do { dma16w(vo1, w1 + (size_t)(64 * kw + 16 * (i)) * F + 32 * jt_, smb + L_W1 + kw * 4096 + (i) * 1024); \
                       dma16w(vo128 ^ (((i) & 1) * 64), w2 + (size_t)(32 * jt_ + 8 * (i)) * 512 + 64 * kw, smb + L_W2 + kw * 4096 + (i) * 1024); } while (0)
    KVRANGE(0, W0);
    cnt_wait(sm, 1, 4);
    KVRANGE(W0, W0 + W1);
    BAR();
    KVRANGE(W0 + W1, 16);
    const v4u Qf = *(LAS v4u*)(sm + L_QS + ((lc >> 2) * 128 + (4 * lg + (lc & 3)) * 8) * 2);
    float sc[8];
    {
      f32x4 d[8];
      waitvm_tie<15>(Kr[0]); d[0] = mfma16(Kr[0], Qf, f32x4{0, 0, 0, 0});
      waitvm_tie<14>(Kr[1]); d[1] = mfma16(Kr[1], Qf, f32x4{0, 0, 0, 0});
      waitvm_tie<13>(Kr[2]); d[2] = mfma16(Kr[2], Qf, f32x4{0, 0, 0, 0});
      waitvm_tie<12>(Kr[3]); d[3] = mfma16(Kr[3], Qf, f32x4{0, 0, 0, 0});
      waitvm_tie<11>(Kr[4]); d[4] = mfma16(Kr[4], Qf, f32x4{0, 0, 0, 0});
      waitvm_tie<10>(Kr[5]); d[5] = mfma16(Kr[5], Qf, f32x4{0, 0, 0, 0});
      waitvm_tie<9>(Kr[6]); d[6] = mfma16(Kr[6], Qf, f32x4{0, 0, 0, 0});
      waitvm_tie<8>(Kr[7]); d[7] = mfma16(Kr[7], Qf, f32x4{0, 0, 0, 0});
#pragma unroll
      for (int i = 0; i < 8; i += 2) {
        float t0 = dpp<0x00>(d[i][0]), t1 = dpp<0x00>(d[i + 1][0]);
        t0 += dpp<0x55>(d[i][1]); t1 += dpp<0x55>(d[i + 1][1]);
        t0 += dpp<0xAA>(d[i][2]); t1 += dpp<0xAA>(d[i + 1][2]);
        t0 += dpp<0xFF>(d[i][3]); t1 += dpp<0xFF>(d[i + 1][3]);
        const unsigned p1 = cvtpk(t0, t1);
        const unsigned p2 = cvtpk(lo2f(p1) * 0.08838834764831845f, hi2f(p1) * 0.08838834764831845f);
        sc[i] = lo2f(p2); sc[i + 1] = hi2f(p2);
      }
    }
    float m = fmaxf(fmaxf(fmaxf(sc[0], sc[1]), fmaxf(sc[2], sc[3])), fmaxf(fmaxf(sc[4], sc[5]), fmaxf(sc[6], sc[7])));
    m = max32(max16(m));
    if (lg == 0 && (lc & 3) == 0) *(LAS float*)(sm + L_SM + (lc >> 2) * 32 + kw * 4) = m;
    BAR();
    {
      const f32x4 m0 = *(LAS f32x4*)(sm + L_SM + (lc >> 2) * 32), m1 = *(LAS f32x4*)(sm + L_SM + (lc >> 2) * 32 + 16);
      m = fmaxf(fmaxf(fmaxf(m0[0], m0[1]), fmaxf(m0[2], m0[3])), fmaxf(fmaxf(m1[0], m1[1]), fmaxf(m1[2], m1[3])));
    }
    const float mneg = -m * 1.4426950408889634f;
    float l = 0.f;
#pragma unroll
    for (int i = 0; i < 8; ++i) { sc[i] = __builtin_amdgcn_exp2f(__builtin_fmaf(sc[i], 1.4426950408889634f, mneg)); l += sc[i]; }
    l = sum32(sum16(l));
    if (lg == 0 && (lc & 3) == 0) *(LAS float*)(sm + L_SL + (lc >> 2) * 32 + kw * 4) = l;
    const int jx = lc & 3;
    const unsigned sA = jx == 0 ? 0x0c0c0100u : jx == 1 ? 0x01000c0cu : 0x0c0c0c0cu;
    const unsigned sB = jx == 2 ? 0x0c0c0100u : jx == 3 ? 0x01000c0cu : 0x0c0c0c0cu;
    const unsigned sAh = jx == 0 ? 0x0c0c0302u : jx == 1 ? 0x03020c0cu : 0x0c0c0c0cu;
    const unsigned sBh = jx == 2 ? 0x0c0c0302u : jx == 3 ? 0x03020c0cu : 0x0c0c0c0cu;
    unsigned X0[8], X1[8];
#pragma unroll
    for (int i = 0; i < 8; i += 2) {
      const unsigned pk = cvtpk(sc[i], sc[i + 1]);
      X0[i] = __builtin_amdgcn_perm(pk, pk, sA); X1[i] = __builtin_amdgcn_perm(pk, pk, sB);
      X0[i + 1] = __builtin_amdgcn_perm(pk, pk, sAh); X1[i + 1] = __builtin_amdgcn_perm(pk, pk, sBh);
    }
    WO_LD(2 * (r >> 4)); WO_LD(2 * (r >> 4) + 1);
    W12_LD(r & 3);
#define PVSTEP(i, n) waitvm_tie<n>(Vr[i]); oa0 = mfma16k16(X0[i], X1[i], Vr[i].x, Vr[i].y, oa0); oa1 = mfma16k16(X0[i], X1[i], Vr[i].z, Vr[i].w, oa1)
    PVSTEP(0, 11); PVSTEP(1, 10); PVSTEP(2, 9); PVSTEP(3, 8); PVSTEP(4, 7); PVSTEP(5, 6); PVSTEP(6, 5); PVSTEP(7, 4);
    *(LAS f32x4*)(sm + L_OP + kw * 2048 + (lg * 128 + lc * 8) * 4) = oa0;
    *(LAS f32x4*)(sm + L_OP + kw * 2048 + (lg * 128 + lc * 8) * 4 + 16) = oa1;
    BAR();
    {
      const int h = kw >> 1, dd = h * 128 + 64 * (kw & 1) + 2 * (lane & 31);
      float a0 = 0.f, a1 = 0.f;
#pragma unroll
      for (int k = 0; k < 4; ++k) {
        const v2u t = *(LAS v2u*)(sm + L_OP + (4 * (lane >> 5) + k) * 2048 + dd * 4);
        a0 += u2f(t.x); a1 += u2f(t.y);
      }
      const f32x4 l0 = *(LAS f32x4*)(sm + L_SL + h * 32), l1 = *(LAS f32x4*)(sm + L_SL + h * 32 + 16);
      const f32x4 mm = *(LAS f32x4*)(sm + L_SM + h * 32), mm1 = *(LAS f32x4*)(sm + L_SM + h * 32 + 16);
      const float L = ((l0[0] + l0[1]) + (l0[2] + l0[3])) + ((l1[0] + l1[1]) + (l1[2] + l1[3]));
      Mh = fmaxf(fmaxf(fmaxf(mm[0], mm[1]), fmaxf(mm[2], mm[3])), fmaxf(fmaxf(mm1[0], mm1[1]), fmaxf(mm1[2], mm1[3])));
      a0 = sum32(a0); a1 = sum32(a1);
      const float rl = frcp(L);
      const unsigned val = nosent(lane < 32 ? cvtpk(a0 * rl, a1 * rl) : f2u((lane & 1) ? L : Mh));
      const int po = lane < 32 ? dd * 2 : BOX_ML + h * 128 + (lane & 31) * 4;
      if (lane < 32 || (kw & 1) == 0) __builtin_amdgcn_raw_buffer_store_b32(val, rs, mybox + po, 0, 16);
    }
    WO_LD(2 * ((r >> 4) ^ 1)); WO_LD(2 * ((r >> 4) ^ 1) + 1);
    waitvm<0>();
  }
  __builtin_amdgcn_s_setprio(3);

  const unsigned tag = *(LAS unsigned*)(sm + L_CNT + 16);
  {
    const int hl = kw >> 1, hf = kw & 1, bq = r >> 4;
    v4u Aw[2][2];
#pragma unroll
    for (int ks = 0; ks < 2; ++ks)
#pragma unroll
      for (int ct = 0; ct < 2; ++ct) {
        const unsigned r0 = 32 * ks + 8 * lg + tq, r1 = r0 + 4, ch = 2 * ct + (tp >> 1);
        Aw[ct][ks] = tr_frag(sm, L_WO + kw * 4096 + r0 * 64 + ((ch ^ ((r0 >> 2) & 2)) * 16) + 8 * (tp & 1), L_WO + kw * 4096 + r1 * 64 + ((ch ^ ((r1 >> 2) & 2)) * 16) + 8 * (tp & 1));
      }
    nap<ADELAY>();
    const int pbox = WS_BOX + (g + 8 * (16 * bq + lc)) * BOX;
    const int offo = pbox + (hl * 128 + hf * 64 + 8 * lg) * 2;
    v4u Bo[2]; v2u ml; unsigned fl = 0; bool flok = false;
    _Pragma("unroll 1") for (int it = 0; it < GSPIN; ++it) {
      RELOAD();
      Bo[0] = __builtin_amdgcn_raw_buffer_load_b128(rs, offo, 0, 16);
      Bo[1] = __builtin_amdgcn_raw_buffer_load_b128(rs, offo + 64, 0, 16);
      ml = __builtin_amdgcn_raw_buffer_load_b64(rs, pbox + BOX_ML + hl * 128, 0, 16);
      if (!flok) fl = __builtin_amdgcn_raw_buffer_load_b32(rs, WS_FLAGS + (kw * 32 + (lane & 31)) * 128, 0, 16);
      flok = __all(fl == tag);
      bool ok = (ml.x != SENT) & (ml.y != SENT) & flok;
#pragma unroll
      for (int k = 0; k < 4; ++k) ok &= (Bo[0][k] != SENT) & (Bo[1][k] != SENT);
      if (__all(ok)) break;
      nap<PSLEEP>();
    }
    const float m_ = u2f(ml.x), l_ = u2f(ml.y);
    const float wgt = l_ * fexp(m_ - quad_max(m_));
    const float f = wgt * frcp(quad_sum(wgt));
#pragma unroll
    for (int ct = 0; ct < 2; ++ct) {
      f32x4 acc = mfma16(Aw[ct][0], Bo[0], f32x4{0, 0, 0, 0});
      acc = mfma16(Aw[ct][1], Bo[1], acc);
#pragma unroll
      for (int k = 0; k < 4; ++k) acc[k] = quad_sum(acc[k] * f);
      if ((lc & 3) == 0) *(LAS f32x4*)(sm + L_RED + kw * 512 + (lc >> 2) * 128 + (16 * ct + 4 * lg) * 4) = acc;
    }
  }
  BAR();
  if (kw == 0) {
    float a0 = 0.f, a1 = 0.f;
#pragma unroll
    for (int k = 0; k < 8; ++k) {
      const v2u t = *(LAS v2u*)(sm + L_RED + k * 512 + lane * 8);
      a0 += u2f(t.x); a1 += u2f(t.y);
    }
    __builtin_amdgcn_raw_buffer_store_b32(nosent(cvtpk(a0, a1)), rs, mybox + BOX_B + lane * 4, 0, 16);
    if (c == 0 && lane == 0) __builtin_amdgcn_raw_buffer_store_b32(tag, rs, WS_EPOCH, 0, 16);
  }
  if (mlp) {
    const unsigned vo128 = (lane >> 3) * 1024 + (((lane & 7) ^ ((lane >> 3) & 2)) * 16);
    const unsigned vo1 = (lane >> 2) * 3584 + (((lane & 3) ^ ((lane >> 4) & 2)) * 16);
    const int jt_ = jt;
#pragma unroll
    for (int p = 1; p < 4; ++p) W12_LD(((r & 3) + p) & 3);
  }
  asm volatile("" ::"v"(owarm));
  nap<BDELAY>();
  float h0, h1;
  {
    const int off = WS_BOX + (8 * (16 * (bp >> 1) + (cp >> 4))) * BOX + BOX_B + (2 * (bp & 1) + hb) * 64 + (cp & 15) * 4;
    unsigned p[8];
    _Pragma("unroll 1") for (int it = 0; it < GSPIN; ++it) {
      RELOAD();
#pragma unroll
      for (int k = 0; k < 8; ++k) p[k] = __builtin_amdgcn_raw_buffer_load_b32(rs, off + k * BOX, 0, 16);
      bool ok = true;
#pragma unroll
      for (int k = 0; k < 8; ++k) ok &= (p[k] != SENT);
      if (__all(ok)) break;
      nap<PSLEEP>();
    }
    float a0 = 0.f, a1 = 0.f;
#pragma unroll
    for (int k = 0; k < 8; ++k) { a0 += lo2f(p[k]); a1 += hi2f(p[k]); }
    const unsigned hp = cvtpk(lo2f(xpair) + rbf(a0), hi2f(xpair) + rbf(a1));
    h0 = lo2f(hp); h1 = hi2f(hp);
    *(LAS unsigned*)(sm + L_HS + hb * 1024 + cp * 4) = hp;
  }
  v4u A1[2][2], A2[4];
  if (mlp) {
    WAITVM(0);
    const float s = half_sum(h0 + h1);
    const float mw = s * (1.f / 64.f);
    const float m2 = half_sum((h0 - mw) * (h0 - mw) + (h1 - mw) * (h1 - mw));
    if ((lane & 31) == 0) *(LAS v2u*)(sm + L_ST + hb * 64 + kw * 8) = v2u{f2u(s), f2u(m2)};
#pragma unroll
    for (int ks = 0; ks < 2; ++ks)
#pragma unroll
      for (int ct = 0; ct < 2; ++ct) {
        const unsigned r0 = 32 * ks + 8 * lg + tq, r1 = r0 + 4, ch = 2 * ct + (tp >> 1);
        A1[ct][ks] = tr_frag(sm, L_W1 + kw * 4096 + r0 * 64 + ((ch ^ ((r0 >> 2) & 2)) * 16) + 8 * (tp & 1), L_W1 + kw * 4096 + r1 * 64 + ((ch ^ ((r1 >> 2) & 2)) * 16) + 8 * (tp & 1));
      }
  }
  BAR();
  if (mlp) {
    {
      f32x4 st[4];
#pragma unroll
      for (int k = 0; k < 4; ++k) st[k] = *(LAS f32x4*)(sm + L_ST + hb * 64 + k * 16);
      const float mean = (((st[0][0] + st[0][2]) + (st[1][0] + st[1][2])) + ((st[2][0] + st[2][2]) + (st[3][0] + st[3][2]))) * (1.f / 512.f);
      float vs = 0.f;
#pragma unroll
      for (int k = 0; k < 4; ++k) {
        const float d0 = st[k][0] * (1.f / 64.f) - mean, d1 = st[k][2] * (1.f / 64.f) - mean;
        vs += (st[k][1] + st[k][3]) + 64.f * (d0 * d0 + d1 * d1);
      }
      const float var = vs * (1.f / 512.f);
      const float mb = rbf(mean);
      const float rden = frcp(rbf(__builtin_amdgcn_sqrtf(rbf(rbf(var) + 1e-5f))));
      *(LAS unsigned*)(sm + L_YS + hb * 1040 + cp * 4) = cvtpk(rbf(h0 - mb) * rden, rbf(h1 - mb) * rden);
    }
#pragma unroll
    for (int ct = 0; ct < 4; ++ct) {
      const unsigned r0 = 8 * lg + tq, r1 = r0 + 4, ch = 4 * (ct >> 1) + tp;
      A2[ct] = tr_frag(sm, L_W2 + kw * 4096 + r0 * 128 + ((ch ^ swz128(r0)) * 16) + 8 * (ct & 1), L_W2 + kw * 4096 + r1 * 128 + ((ch ^ swz128(r1)) * 16) + 8 * (ct & 1));
    }
    asm volatile("s_waitcnt lgkmcnt(0)" ::: "memory");
    {
      f32x4 acc[2] = {{0, 0, 0, 0}, {0, 0, 0, 0}};
#pragma unroll
      for (int ks = 0; ks < 2; ++ks) {
        const v4u Bf = *(LAS v4u*)(sm + L_YS + (lc & 1) * 1040 + (64 * kw + 32 * ks + 8 * lg) * 2);
        acc[0] = mfma16(A1[0][ks], Bf, acc[0]);
        acc[1] = mfma16(A1[1][ks], Bf, acc[1]);
      }
      if (lc < 2) {
        *(LAS f32x4*)(sm + L_RED2 + kw * 256 + lc * 128 + (4 * lg) * 4) = acc[0];
        *(LAS f32x4*)(sm + L_RED2 + kw * 256 + lc * 128 + (16 + 4 * lg) * 4) = acc[1];
      }
    }
    BAR();
    {
      float z = 0.f;
#pragma unroll
      for (int k = 0; k < 8; ++k) z += *(LAS float*)(sm + L_RED2 + k * 256 + lane * 4);
      z = rbf(rbf(z) + bf2f(b1v));
      const float u = 0.7978845608028654f * (z + 0.044715f * z * z * z);
      const float th = 1.f - 2.f * frcp(fexp(2.f * u) + 1.f);
      const float gl = 0.5f * z * (1.f + th);
      *(LAS u16*)(sm + L_GS + kw * 128 + lane * 2) = (u16)(cvtpk(gl, gl) & 0xffffu);
      asm volatile("s_waitcnt lgkmcnt(0)" ::: "memory");
      const v4u Bf = *(LAS v4u*)(sm + L_GS + kw * 128 + (lc & 1) * 64 + lg * 16);
      f32x4 o[4];
#pragma unroll
      for (int ct = 0; ct < 4; ++ct) o[ct] = mfma16(A2[ct], Bf, f32x4{0, 0, 0, 0});
      if (lc < 4) {
        const f32x4 oa = lc < 2 ? o[0] : o[2], ob = lc < 2 ? o[1] : o[3];
        const v4u t = {nosent(cvtpk(oa[0], oa[1])), nosent(cvtpk(oa[2], oa[3])), nosent(cvtpk(ob[0], ob[1])), nosent(cvtpk(ob[2], ob[3]))};
        __builtin_amdgcn_raw_buffer_store_b128(t, rs, mybox + BOX_C + (lc & 1) * 1024 + (64 * kw + 32 * (lc >> 1) + 8 * lg) * 2, 0, 16);
      }
    }
  }
  if (!mlp && kw < 2) {
    nap<CDELAY>();
    const int o0 = WS_BOX + ((lane >> 3) + 8 * bp) * BOX + BOX_C + fb * 1024 + 128 * g + (lane & 7) * 16;
    v4u p[7];
    _Pragma("unroll 1") for (int it = 0; it < GSPIN; ++it) {
      RELOAD();
#pragma unroll
      for (int k = 0; k < 7; ++k) p[k] = __builtin_amdgcn_raw_buffer_load_b128(rs, o0 + k * (32 * BOX), 0, 16);
      bool ok = true;
#pragma unroll
      for (int k = 0; k < 7; ++k) ok &= (p[k][0] != SENT) & (p[k][1] != SENT) & (p[k][2] != SENT) & (p[k][3] != SENT);
      if (__all(ok)) break;
      nap<PSLEEP>();
    }
    float v[8];
#pragma unroll
    for (int k = 0; k < 4; ++k) {
      float a = 0.f, bb = 0.f;
#pragma unroll
      for (int j = 0; j < 7; ++j) { a += lo2f(p[j][k]); bb += hi2f(p[j][k]); }
      v[2 * k] = a; v[2 * k + 1] = bb;
    }
    float u4[4];
#pragma unroll
    for (int k = 0; k < 4; ++k) { float a = v[k], bb = v[k + 4]; swap32(a, bb); u4[k] = a + bb; }
    float u2[2];
#pragma unroll
    for (int k = 0; k < 2; ++k) { float a = u4[k], bb = u4[k + 2]; swap16(a, bb); u2[k] = a + bb; }
#pragma unroll
    for (int k = 0; k < 2; ++k) u2[k] += dpp<0x128>(u2[k]);
    const unsigned hp = *(LAS unsigned*)(sm + L_HS + fb * 1024 + fcol * 2);
    const float r0 = rbf(rbf(u2[0]) + lo2f(b2pair)), r1 = rbf(rbf(u2[1]) + hi2f(b2pair));
    const unsigned res = cvtpk(lo2f(hp) + r0, hi2f(hp) + r1);
    if ((lane & 8) == 0) __builtin_amdgcn_raw_buffer_store_b32(res, ro, ooff, 0, 16);
  }
  return;
  }
  {
    __builtin_amdgcn_s_setprio(3);
    const int kq = r >> 3, cb = r & 7;
    const unsigned E = __builtin_amdgcn_raw_buffer_load_b32(rs, 0, WS_EPOCH, 16);
    v4u xr[2];
#pragma unroll
    for (int i = 0; i < 2; ++i) xr[i] = *(const v4u*)(x + (2 * w + i) * D + lane * 8);
    {
      const unsigned vo = (lane >> 3) * 8192 + (((lane & 7) ^ ((lane >> 3) & 2)) * 16);
#pragma unroll
      for (int i = 0; i < 4; ++i)
        dma16s(vo ^ ((i & 1) * 64), wq + (size_t)(128 * kq + 32 * w + 8 * i) * 4096 + 512 * g + 64 * cb, smb + L_WQ + (32 * w + 8 * i) * 128);
    }
    {
      const int idx = w * 64 + lane;
      if (idx < BOX / 16) __builtin_amdgcn_raw_buffer_store_b128(v4u{SENT, SENT, SENT, SENT}, rs, mybox + idx * 16, 0, 16);
    }
    if (tid < 2) *((LAS unsigned*)(sm + L_CNT) + tid) = 0;
    BAR();
    const unsigned tag = __builtin_amdgcn_readfirstlane(E) + 1u;
#pragma unroll
    for (int i = 0; i < 2; ++i) {
      float v[8];
#pragma unroll
      for (int k = 0; k < 4; ++k) { v[2 * k] = lo2f(xr[i][k]); v[2 * k + 1] = hi2f(xr[i][k]); }
      float s = 0.f;
#pragma unroll
      for (int k = 0; k < 8; ++k) s += v[k];
      const float mean = wave_sum(s) * (1.f / 512.f);
      float q2 = 0.f;
#pragma unroll
      for (int k = 0; k < 8; ++k) { const float d = v[k] - mean; q2 += d * d; }
      const float var = wave_sum(q2) * (1.f / 512.f);
      const float mb = rbf(mean);
      const float rden = frcp(rbf(__builtin_amdgcn_sqrtf(rbf(rbf(var) + 1e-5f))));
      v4u y;
#pragma unroll
      for (int k = 0; k < 4; ++k) y[k] = cvtpk(rbf(v[2 * k] - mb) * rden, rbf(v[2 * k + 1] - mb) * rden);
      if (lg == kq) *(LAS v4u*)(sm + L_XN + (2 * w + i) * 272 + lc * 16) = y;
    }
    WAITVM(0);
    cnt_add(sm, 0, lane);
    cnt_wait(sm, 0, 4);
    if (tid == 0) {
      __builtin_amdgcn_raw_buffer_store_b32(tag, rs, WS_FLAGS + c * 128, 0, 16);
      *(LAS unsigned*)(sm + L_CNT + 16) = tag;
    }
    {
      f32x4 acc = {0, 0, 0, 0};
#pragma unroll
      for (int ks = 0; ks < 4; ++ks) {
        const unsigned r0 = 32 * ks + 8 * lg + tq, r1 = r0 + 4, ch = 2 * w + (tp >> 1);
        const v4u A = tr_frag(sm, L_WQ + r0 * 128 + ((ch ^ swz128(r0)) * 16) + 8 * (tp & 1), L_WQ + r1 * 128 + ((ch ^ swz128(r1)) * 16) + 8 * (tp & 1));
        const v4u Bf = *(LAS v4u*)(sm + L_XN + (lc & 7) * 272 + (32 * ks + 8 * lg) * 2);
        acc = mfma16(A, Bf, acc);
      }
      {
        const int off = WS_Q + ((((g * 8 + (lc & 7)) * 4 + kq) * 512) + 64 * cb + 16 * w + 4 * lg) * 8 + (lc >> 3) * 16;
        const float e0 = lc < 8 ? acc[0] : acc[2], e1 = lc < 8 ? acc[1] : acc[3];
        __builtin_amdgcn_raw_buffer_store_b128(v4u{f2u(e0), tag, f2u(e1), tag}, rs, off, 0, 16);
      }
    }
    nap<QDELAY>();
    {
      const int b = r >> 2, ht = w * 64 + lane;
      const int off = WS_Q + ((g * 8 + b) * 4 * 512 + 2 * ht) * 8;
      v4u q[4];
#define QOK(q) __all(((q)[0].y == tag) & ((q)[0].w == tag) & ((q)[1].y == tag) & ((q)[1].w == tag) & ((q)[2].y == tag) & ((q)[2].w == tag) & ((q)[3].y == tag) & ((q)[3].w == tag))
      _Pragma("unroll 1") for (int it = 0; it < GSPIN; ++it) {
        RELOAD();
#pragma unroll
        for (int k = 0; k < 4; ++k) q[k] = __builtin_amdgcn_raw_buffer_load_b128(rs, off + k * 4096, 0, 16);
        if (it == 0) cnt_add(sm, 1, lane);
        if (QOK(q)) break;
        nap<PSLEEP>();
      }
      float q0 = 0.f, q1 = 0.f;
#pragma unroll
      for (int k = 0; k < 4; ++k) { q0 += u2f(q[k].x); q1 += u2f(q[k].z); }
      *(LAS unsigned*)(sm + L_QS + ht * 4) = cvtpk(q0, q1);
    }
    BAR();
  }

}

extern "C" void kernel_launch(void* const* d_in, const int* in_sizes, int n_in, void* d_out, int out_size, void* d_ws,
                              size_t ws_size, hipStream_t stream) {
  fused_block<<<256, 768, 0, stream>>>((const u16*)d_in[0], (const u16*)d_in[1], (const u16*)d_in[2], (const u16*)d_in[3],
                                       (const u16*)d_in[4], (const u16*)d_in[6], (const u16*)d_in[7], (const u16*)d_in[8],
                                       (const u16*)d_in[9], (u16*)d_out, (char*)d_ws);
}
```
